```python
import torch

def _softmax(x, axis=-1):
    return torch.softmax(x, dim=axis)
def _gelu(x):
    return torch.nn.functional.gelu(x, approximate="tanh")
def _silu(x):
    return torch.nn.functional.silu(x)
def _sigmoid(x):
    return torch.sigmoid(x)
def _layernorm(x, axis=-1, eps=1e-5):
    m = x.mean(dim=axis, keepdim=True)
    v = x.var(dim=axis, keepdim=True, unbiased=False)
    return (x - m) / torch.sqrt(v + eps)
def _scatter_add(tgt, idx, src, axis=0):
    return tgt.clone().index_add_(axis, idx, src)
def _segment_sum(src, seg, n):
    out = torch.zeros((n,) + src.shape[1:], dtype=src.dtype)
    return out.index_add_(0, seg, src)
def _pad(x, pw):
    flat = tuple(v for pair in reversed(pw) for v in pair)
    return torch.nn.functional.pad(x, flat)

kernel_name = 'recurrent_scan__gqa_block_cached__mixer_channel_mlp__decode'

def setup_inputs():

    _w = (lambda a: (lambda t: t.to(torch.bfloat16) if t.is_floating_point() else t)(torch.as_tensor(a)))
    def _A_setup():
        B, Sq, D = 1, 1, 640
        Hq, Hkv, Sk, Dh = 32, 32, 8192, 192
        assert Hq % Hkv == 0, (Hq, Hkv)
        return {
            "x": _w((lambda *s: torch.randn(*s, dtype=torch.bfloat16) * 0.05)(B, Sq, D)),
            "k_cache": _w((lambda *s: torch.randn(*s, dtype=torch.bfloat16) * 1.6)(B, Hkv, Sk, Dh)),
            "v_cache": _w((lambda *s: torch.randn(*s, dtype=torch.bfloat16) * 1.6)(B, Hkv, Sk, Dh)),
            "wq": _w((lambda *s: torch.randn(*s, dtype=torch.bfloat16) * 1.6)(D, Hq * Dh) * (D ** -0.5)),
            "wo": _w((lambda *s: torch.randn(*s, dtype=torch.bfloat16) * 1.6)(Hq * Dh, D) * ((Hq * Dh) ** -0.5)),
        }
    _A_in = _A_setup()

    def _B_setup():
        B, S, D, F = 1, 1, 640, 1536
        return {
            "x": _w((lambda *s: torch.randn(*s, dtype=torch.bfloat16) * 0.05)(B, S, D)),
            "w1": _w((lambda *s: torch.randn(*s, dtype=torch.bfloat16) * 0.8)(D, F) * (D ** -0.5)),
            "b1": _w((lambda *s: torch.randn(*s, dtype=torch.bfloat16) * 0.4)(F)),
            "w2": _w((lambda *s: torch.randn(*s, dtype=torch.bfloat16) * 0.4)(F, D) * (F ** -0.5)),
            "b2": _w((lambda *s: torch.randn(*s, dtype=torch.bfloat16) * 0.2)(D)),
        }
    _B_in = _B_setup()

    return {
        "A_x": _A_in["x"],
        "A_k_cache": _A_in["k_cache"],
        "A_v_cache": _A_in["v_cache"],
        "A_wq": _A_in["wq"],
        "A_wo": _A_in["wo"],
        "B_x": _B_in["x"],
        "B_w1": _B_in["w1"],
        "B_b1": _B_in["b1"],
        "B_w2": _B_in["w2"],
        "B_b2": _B_in["b2"],
    }

def reference(A_x, A_k_cache, A_v_cache, A_wq, A_wo, B_x, B_w1, B_b1, B_w2, B_b2):
    def _A_ref(x, k_cache, v_cache, wq, wo):
        B, Sq, D = x.shape
        Hq, Dh = 32, 192
        Hkv = k_cache.shape[1]
        rep = Hq // Hkv
        q = (lambda x, *d: x.permute(*d))((x @ wq).reshape(B, Sq, Hq, Dh), 0, 2, 1, 3)
        k = (lambda x, n, axis: torch.repeat_interleave(x, n, dim=axis))(k_cache, rep, axis=1)
        v = (lambda x, n, axis: torch.repeat_interleave(x, n, dim=axis))(v_cache, rep, axis=1)
        scores = torch.einsum("bhqd,bhkd->bhqk", q, k) * (Dh ** -0.5)
        p = _softmax(scores, axis=-1)
        o = torch.einsum("bhqk,bhkd->bhqd", p, v)
        return (lambda x, *d: x.permute(*d))(o, 0, 2, 1, 3).reshape(B, Sq, Hq * Dh) @ wo

    def _B_ref(x, w1, b1, w2, b2):
        y = _layernorm(x, axis=-1)
        h = _gelu(torch.einsum("bsd,df->bsf", y, w1) + b1)
        return torch.einsum("bsf,fd->bsd", h, w2) + b2

    x = A_x
    xn = _layernorm(x, axis=-1)
    s = _A_ref(xn, A_k_cache, A_v_cache, A_wq, A_wo)
    g = _silu(_B_ref(xn, B_w1, B_b1, B_w2, B_b2))
    return x + s * g
```

```cpp
#include <hip/hip_runtime.h>
#include <hip/hip_bf16.h>
#include <stdint.h>

#define KW 8
#define KV_NT 1

namespace {

constexpr int DM = 640, NH = 32, SK = 8192, DH = 192, FF = 1536;
constexpr int NSPLIT = 8, NTHR = 512, NWAVE = 8;
constexpr int KW0 = 4;
constexpr int NG = 16;
constexpr int H1N = 200;
constexpr int H2N = 160;
constexpr size_t WS_EPOCH = 0, WS_H1 = 4096, WS_H2 = WS_H1 + 256 * 2048, WS_END = WS_H2 + 256 * 2048;

using u32x4 = __attribute__((ext_vector_type(4))) unsigned;
using u32x3 = __attribute__((ext_vector_type(3))) unsigned;
using f32x4 = __attribute__((ext_vector_type(4))) float;
using bf16x2 = __attribute__((ext_vector_type(2))) __bf16;
typedef __attribute__((address_space(1))) unsigned long long gu64;
typedef __attribute__((address_space(1))) unsigned gu32;

__device__ __forceinline__ float rbf(float x) { return (float)(__bf16)x; }
__device__ __forceinline__ float bflo(unsigned u) { return __uint_as_float(u << 16); }
__device__ __forceinline__ float bfhi(unsigned u) { return __uint_as_float(u & 0xffff0000u); }
__device__ __forceinline__ float bf1(unsigned short u) { return __uint_as_float((unsigned)u << 16); }
__device__ __forceinline__ float dot2(unsigned k, unsigned q, float acc) {
  return __builtin_amdgcn_fdot2_f32_bf16(__builtin_bit_cast(bf16x2, k), __builtin_bit_cast(bf16x2, q), acc, false);
}
template <int CTRL> __device__ __forceinline__ float dpp(float x) {
  return __builtin_bit_cast(float, __builtin_amdgcn_mov_dpp(__builtin_bit_cast(int, x), CTRL, 0xf, 0xf, true));
}
constexpr int XOR1 = 0xB1, XOR2 = 0x4E, XOR7 = 0x141, XOR8 = 0x128;
__device__ __forceinline__ float x16_32_sum(float x) {
  auto s = __builtin_amdgcn_permlane16_swap(__float_as_uint(x), __float_as_uint(x), false, false);
  x = __uint_as_float(s[0]) + __uint_as_float(s[1]);
  auto t = __builtin_amdgcn_permlane32_swap(__float_as_uint(x), __float_as_uint(x), false, false);
  return __uint_as_float(t[0]) + __uint_as_float(t[1]);
}
__device__ __forceinline__ float x16_32_max(float x) {
  auto s = __builtin_amdgcn_permlane16_swap(__float_as_uint(x), __float_as_uint(x), false, false);
  x = fmaxf(__uint_as_float(s[0]), __uint_as_float(s[1]));
  auto t = __builtin_amdgcn_permlane32_swap(__float_as_uint(x), __float_as_uint(x), false, false);
  return fmaxf(__uint_as_float(t[0]), __uint_as_float(t[1]));
}
__device__ __forceinline__ float sum8(float x) { x += dpp<XOR1>(x); x += dpp<XOR2>(x); x += dpp<XOR7>(x); return x; }
__device__ __forceinline__ float sumrg(float x) { x += dpp<XOR8>(x); return x16_32_sum(x); }
__device__ __forceinline__ float maxrg(float x) { x = fmaxf(x, dpp<XOR8>(x)); return x16_32_max(x); }
__device__ __forceinline__ float sum64(float x) { return sumrg(sum8(x)); }

__device__ __forceinline__ u32x4 ldkv(const u32x4* p) {
  return __builtin_nontemporal_load(p);
}
__device__ __forceinline__ void st_granule(gu64* g, unsigned tag, float v) {
  __hip_atomic_store(g, ((unsigned long long)tag << 32) | (unsigned long long)__float_as_uint(v), __ATOMIC_RELAXED, __HIP_MEMORY_SCOPE_AGENT);
}
__device__ __forceinline__ unsigned long long ld_granule(gu64* g) {
  return __hip_atomic_load(g, __ATOMIC_RELAXED, __HIP_MEMORY_SCOPE_AGENT);
}
__device__ __forceinline__ void wg_barrier() {
  asm volatile("s_waitcnt lgkmcnt(0)" ::: "memory");
  __builtin_amdgcn_s_barrier();
  asm volatile("" ::: "memory");
}

constexpr unsigned SPIN_CAP = 1u << 22;

constexpr int L_XN = 0;
constexpr int L_Y = 640;
constexpr int L_PART = 1280;
constexpr int L_ML = L_PART + 8 * 192;
constexpr int L_HP = L_ML + 32;
constexpr int L_QB = L_HP + 64;
constexpr int L_G1 = L_QB + 96;
constexpr int L_OS = L_G1 + 8 * H1N;
constexpr int L_RED = L_OS + 256;
constexpr int L_FIN = L_RED + 48 * 160;
constexpr int L_END = L_FIN + 160;

__global__ void __launch_bounds__(NTHR, 2)
fused_decode_kernel(const unsigned short* __restrict__ x, const unsigned short* __restrict__ kc, const unsigned short* __restrict__ vc,
                    const unsigned short* __restrict__ wq, const unsigned short* __restrict__ wo, const unsigned short* __restrict__ w1,
                    const unsigned short* __restrict__ b1, const unsigned short* __restrict__ w2, const unsigned short* __restrict__ b2,
                    unsigned short* __restrict__ out, unsigned char* ws) {
  __shared__ __attribute__((aligned(16))) float lds[L_END];
  const int b = blockIdx.x, xcd = b & 7, hq = (b >> 3) & 3, j = b >> 5, hd = xcd * 4 + hq;
  const int tid = threadIdx.x, lane = tid & 63, wave = __builtin_amdgcn_readfirstlane(tid >> 6);
  const int rg = lane >> 3, cl = lane & 7;

  unsigned xw[5];
  if (wave == 0) {
#pragma unroll
    for (int i = 0; i < 5; ++i) xw[i] = ((const unsigned*)x)[lane + 64 * i];
  }
  const unsigned E = __hip_atomic_load((gu32*)(ws + WS_EPOCH), __ATOMIC_RELAXED, __HIP_MEMORY_SCOPE_AGENT);
  u32x4 wqv[10][3];
  {
    const u32x4* p = (const u32x4*)(wq + (size_t)(80 * wave + rg) * (NH * DH) + hd * DH + 8 * cl);
#pragma unroll
    for (int t = 0; t < 10; ++t)
#pragma unroll
      for (int i = 0; i < 3; ++i) wqv[t][i] = p[(size_t)t * (8 * NH * DH / 8) + 8 * i];
  }
  u32x3 w1v[2];
  if (wave < 5) {
#pragma unroll
    for (int i = 0; i < 2; ++i) w1v[i] = *(const u32x3*)(w1 + (size_t)(128 * wave + 64 * i + lane) * FF + hd * 48 + j * 6);
  }
  const u32x4* kp = (const u32x4*)(kc + ((size_t)hd * SK + j * 1024 + wave * 128 + rg) * DH + 8 * cl);
  const u32x4* vp = (const u32x4*)(vc + ((size_t)hd * SK + j * 1024 + wave * 128 + rg) * DH + 8 * cl);
  u32x4 buf[KW][3];
#pragma unroll
  for (int g = 0; g < KW0; ++g)
#pragma unroll
    for (int i = 0; i < 3; ++i) buf[g][i] = ldkv(kp + g * 192 + 8 * i);

  if (wave == 0) {
    float v[10];
#pragma unroll
    for (int i = 0; i < 5; ++i) { v[2 * i] = bflo(xw[i]); v[2 * i + 1] = bfhi(xw[i]); }
    float s = 0.f;
#pragma unroll
    for (int i = 0; i < 10; ++i) s += v[i];
    float mean = sum64(s) * (1.f / DM), q = 0.f;
#pragma unroll
    for (int i = 0; i < 10; ++i) { const float d = v[i] - mean; q += d * d; }
    float var = rbf(sum64(q) * (1.f / DM));
    float den = rbf(sqrtf(rbf(var + 1e-5f))), mb = rbf(mean);
#pragma unroll
    for (int i = 0; i < 10; ++i) v[i] = rbf(rbf(v[i] - mb) / den);
#pragma unroll
    for (int i = 0; i < 5; ++i) { lds[L_XN + 2 * (lane + 64 * i)] = v[2 * i]; lds[L_XN + 2 * (lane + 64 * i) + 1] = v[2 * i + 1]; }
    s = 0.f;
#pragma unroll
    for (int i = 0; i < 10; ++i) s += v[i];
    mean = sum64(s) * (1.f / DM); q = 0.f;
#pragma unroll
    for (int i = 0; i < 10; ++i) { const float d = v[i] - mean; q += d * d; }
    var = rbf(sum64(q) * (1.f / DM));
    den = rbf(sqrtf(rbf(var + 1e-5f))); mb = rbf(mean);
#pragma unroll
    for (int i = 0; i < 10; ++i) v[i] = rbf(rbf(v[i] - mb) / den);
#pragma unroll
    for (int i = 0; i < 5; ++i) { lds[L_Y + 2 * (lane + 64 * i)] = v[2 * i]; lds[L_Y + 2 * (lane + 64 * i) + 1] = v[2 * i + 1]; }
  }
  wg_barrier();

  {
    float acc[24];
#pragma unroll
    for (int k = 0; k < 24; ++k) acc[k] = 0.f;
#pragma unroll
    for (int t = 0; t < 10; ++t) {
      const float xv = lds[L_XN + 80 * wave + 8 * t + rg];
#pragma unroll
      for (int i = 0; i < 3; ++i)
#pragma unroll
        for (int c = 0; c < 4; ++c) {
          acc[i * 8 + 2 * c] = fmaf(xv, bflo(wqv[t][i][c]), acc[i * 8 + 2 * c]);
          acc[i * 8 + 2 * c + 1] = fmaf(xv, bfhi(wqv[t][i][c]), acc[i * 8 + 2 * c + 1]);
        }
    }
#pragma unroll
    for (int k = 0; k < 24; ++k) acc[k] = sumrg(acc[k]);
    if (rg == 0) {
#pragma unroll
      for (int i = 0; i < 3; ++i) {
        *(f32x4*)&lds[L_PART + wave * 192 + 8 * (cl + 8 * i)] = f32x4{acc[i * 8], acc[i * 8 + 1], acc[i * 8 + 2], acc[i * 8 + 3]};
        *(f32x4*)&lds[L_PART + wave * 192 + 8 * (cl + 8 * i) + 4] = f32x4{acc[i * 8 + 4], acc[i * 8 + 5], acc[i * 8 + 6], acc[i * 8 + 7]};
      }
    }
    if (wave < 5) {
      float hp[6];
      const float y0 = lds[L_Y + 128 * wave + lane], y1 = lds[L_Y + 128 * wave + 64 + lane];
#pragma unroll
      for (int c = 0; c < 3; ++c) {
        hp[2 * c] = sum64(y0 * bflo(w1v[0][c]) + y1 * bflo(w1v[1][c]));
        hp[2 * c + 1] = sum64(y0 * bfhi(w1v[0][c]) + y1 * bfhi(w1v[1][c]));
      }
      if (lane == 0) {
#pragma unroll
        for (int c = 0; c < 6; ++c) lds[L_HP + wave * 8 + c] = hp[c];
      }
    }
  }
#pragma unroll
  for (int g = KW0; g < KW; ++g)
#pragma unroll
    for (int i = 0; i < 3; ++i) buf[g][i] = ldkv(kp + g * 192 + 8 * i);
  u32x4 wov[4], w2v;
  const int cg = tid % 10, rr = tid / 10;
  if (tid < 480) {
#pragma unroll
    for (int k = 0; k < 4; ++k) wov[k] = *(const u32x4*)(wo + (size_t)(hd * DH + rr + 48 * k) * DM + 80 * j + 8 * cg);
    w2v = *(const u32x4*)(w2 + (size_t)(hd * 48 + rr) * DM + 80 * j + 8 * cg);
  }
  wg_barrier();
  const unsigned tag = E + 1u;
  gu64* h1 = (gu64*)(ws + WS_H1);
  gu64* h2 = (gu64*)(ws + WS_H2);
  if (tid < 192) {
    float s = 0.f;
#pragma unroll
    for (int w = 0; w < 8; ++w) s += lds[L_PART + w * 192 + tid];
    ((__bf16*)&lds[L_QB])[tid] = (__bf16)s;
  } else if (tid < 198) {
    const int c = tid - 192;
    float s = 0.f;
#pragma unroll
    for (int w = 0; w < 5; ++w) s += lds[L_HP + w * 8 + c];
    const float pre = rbf(rbf(s) + bf1(b1[hd * 48 + j * 6 + c]));
    const float u = 0.7978845608028654f * (pre + 0.044715f * pre * pre * pre);
    const float hv = rbf(0.5f * pre * (1.f + tanhf(u)));
    st_granule(h1 + (size_t)b * 256 + 194 + c, tag, hv);
  }
  wg_barrier();
  u32x4 qv[3];
#pragma unroll
  for (int i = 0; i < 3; ++i) qv[i] = *(const u32x4*)&lds[L_QB + 4 * (cl + 8 * i)];

  float s[NG];
#pragma unroll
  for (int g = 0; g < NG; ++g) {
    float d = 0.f;
#pragma unroll
    for (int i = 0; i < 3; ++i)
#pragma unroll
      for (int c = 0; c < 4; ++c) d = dot2(buf[g % KW][i][c], qv[i][c], d);
    const int gn = g + KW;
#pragma unroll
    for (int i = 0; i < 3; ++i) buf[g % KW][i] = gn < NG ? ldkv(kp + gn * 192 + 8 * i) : ldkv(vp + (gn - NG) * 192 + 8 * i);
    d = sum8(d);
    s[g] = rbf(rbf(d) * 0.07216878364870322f);
  }
  float mx = s[0];
#pragma unroll
  for (int g = 1; g < NG; ++g) mx = fmaxf(mx, s[g]);
  mx = maxrg(mx);
  float ls = 0.f;
#pragma unroll
  for (int g = 0; g < NG; ++g) { s[g] = __builtin_amdgcn_exp2f((s[g] - mx) * 1.4426950408889634f); ls += s[g]; }
  ls = sumrg(ls);

  float acc[24];
#pragma unroll
  for (int k = 0; k < 24; ++k) acc[k] = 0.f;
#pragma unroll
  for (int g = 0; g < NG; ++g) {
    const int G = g + NG;
    const float p = s[g];
#pragma unroll
    for (int i = 0; i < 3; ++i)
#pragma unroll
      for (int c = 0; c < 4; ++c) {
        acc[i * 8 + 2 * c] = fmaf(p, bflo(buf[G % KW][i][c]), acc[i * 8 + 2 * c]);
        acc[i * 8 + 2 * c + 1] = fmaf(p, bfhi(buf[G % KW][i][c]), acc[i * 8 + 2 * c + 1]);
      }
    if (g + KW < NG) {
#pragma unroll
      for (int i = 0; i < 3; ++i) buf[G % KW][i] = ldkv(vp + (g + KW) * 192 + 8 * i);
    }
  }
#pragma unroll
  for (int k = 0; k < 24; ++k) acc[k] = sumrg(acc[k]);
  if (rg == 0) {
#pragma unroll
    for (int i = 0; i < 3; ++i) {
      *(f32x4*)&lds[L_PART + wave * 192 + 8 * (cl + 8 * i)] = f32x4{acc[i * 8], acc[i * 8 + 1], acc[i * 8 + 2], acc[i * 8 + 3]};
      *(f32x4*)&lds[L_PART + wave * 192 + 8 * (cl + 8 * i) + 4] = f32x4{acc[i * 8 + 4], acc[i * 8 + 5], acc[i * 8 + 6], acc[i * 8 + 7]};
    }
    if (cl == 0) { lds[L_ML + wave * 2] = mx; lds[L_ML + wave * 2 + 1] = ls; }
  }
  wg_barrier();
  if (tid < 194) {
    float M = lds[L_ML];
#pragma unroll
    for (int w = 1; w < 8; ++w) M = fmaxf(M, lds[L_ML + 2 * w]);
    float r = 0.f;
    if (tid < 192) {
#pragma unroll
      for (int w = 0; w < 8; ++w) r = fmaf(__builtin_amdgcn_exp2f((lds[L_ML + 2 * w] - M) * 1.4426950408889634f), lds[L_PART + w * 192 + tid], r);
    } else if (tid == 192) {
      r = M;
    } else {
#pragma unroll
      for (int w = 0; w < 8; ++w) r = fmaf(__builtin_amdgcn_exp2f((lds[L_ML + 2 * w] - M) * 1.4426950408889634f), lds[L_ML + 2 * w + 1], r);
    }
    st_granule(h1 + (size_t)b * 256 + tid, tag, r);
  }
  {
    gu64* src = h1 + (size_t)(wave * 32 + hq * 8 + xcd) * 256;
    unsigned long long g0, g1, g2, g3;
    for (unsigned spins = 0; spins < SPIN_CAP; ++spins) {
      g0 = ld_granule(src + lane); g1 = ld_granule(src + 64 + lane); g2 = ld_granule(src + 128 + lane);
      g3 = lane < 8 ? ld_granule(src + 192 + lane) : ((unsigned long long)tag << 32);
      const bool ok = (g0 >> 32) == tag && (g1 >> 32) == tag && (g2 >> 32) == tag && (g3 >> 32) == tag;
      if (__all(ok)) break;
      __builtin_amdgcn_s_sleep(1);
    }
    lds[L_G1 + wave * H1N + lane] = __uint_as_float((unsigned)g0);
    lds[L_G1 + wave * H1N + 64 + lane] = __uint_as_float((unsigned)g1);
    lds[L_G1 + wave * H1N + 128 + lane] = __uint_as_float((unsigned)g2);
    if (lane < 8) lds[L_G1 + wave * H1N + 192 + lane] = __uint_as_float((unsigned)g3);
  }
  wg_barrier();
  if (tid < 192) {
    float M = lds[L_G1 + 192];
#pragma unroll
    for (int w = 1; w < 8; ++w) M = fmaxf(M, lds[L_G1 + w * H1N + 192]);
    float L = 0.f, o = 0.f;
#pragma unroll
    for (int w = 0; w < 8; ++w) {
      const float e = __builtin_amdgcn_exp2f((lds[L_G1 + w * H1N + 192] - M) * 1.4426950408889634f);
      L = fmaf(e, lds[L_G1 + w * H1N + 193], L);
      o = fmaf(e, lds[L_G1 + w * H1N + tid], o);
    }
    lds[L_OS + tid] = rbf(o / L);
  } else if (tid < 240) {
    const int c = tid - 192;
    lds[L_OS + tid] = lds[L_G1 + (c / 6) * H1N + 194 + (c % 6)];
  }
  wg_barrier();
  if (tid < 480) {
    float a[8], m[8];
    const float hv = lds[L_OS + 192 + rr];
#pragma unroll
    for (int c = 0; c < 4; ++c) { m[2 * c] = hv * bflo(w2v[c]); m[2 * c + 1] = hv * bfhi(w2v[c]); a[2 * c] = 0.f; a[2 * c + 1] = 0.f; }
#pragma unroll
    for (int k = 0; k < 4; ++k) {
      const float ov = lds[L_OS + rr + 48 * k];
#pragma unroll
      for (int c = 0; c < 4; ++c) { a[2 * c] = fmaf(ov, bflo(wov[k][c]), a[2 * c]); a[2 * c + 1] = fmaf(ov, bfhi(wov[k][c]), a[2 * c + 1]); }
    }
    float* r = &lds[L_RED + rr * 160 + 8 * cg];
    *(f32x4*)r = f32x4{a[0], a[1], a[2], a[3]}; *(f32x4*)(r + 4) = f32x4{a[4], a[5], a[6], a[7]};
    *(f32x4*)(r + 80) = f32x4{m[0], m[1], m[2], m[3]}; *(f32x4*)(r + 84) = f32x4{m[4], m[5], m[6], m[7]};
  }
  wg_barrier();
  if (tid < H2N) {
    float r = 0.f;
#pragma unroll
    for (int k = 0; k < 48; ++k) r += lds[L_RED + k * 160 + tid];
    st_granule(h2 + (size_t)(hd * 8 + j) * 256 + tid, tag, r);
  }
  if (hd != 4 * j) return;
  wg_barrier();
  {
    unsigned long long g[10];
    for (unsigned spins = 0; spins < SPIN_CAP; ++spins) {
      bool ok = true;
#pragma unroll
      for (int k = 0; k < 10; ++k) {
        const int idx = lane + 64 * k, pr = 4 * wave + idx / 160, e = idx % 160;
        g[k] = ld_granule(h2 + (size_t)(pr * 8 + j) * 256 + e);
        ok &= (g[k] >> 32) == tag;
      }
      if (__all(ok)) break;
      __builtin_amdgcn_s_sleep(1);
    }
#pragma unroll
    for (int k = 0; k < 10; ++k) lds[L_RED + wave * 640 + lane + 64 * k] = __uint_as_float((unsigned)g[k]);
  }
  wg_barrier();
  if (tid < H2N) {
    float r = 0.f;
#pragma unroll
    for (int k = 0; k < 32; ++k) r += lds[L_RED + k * 160 + tid];
    lds[L_FIN + tid] = r;
  }
  wg_barrier();
  if (tid < 80) {
    const int col = 80 * j + tid;
    const float sv = rbf(lds[L_FIN + tid]);
    const float mv = rbf(rbf(lds[L_FIN + 80 + tid]) + bf1(b2[col]));
    const float gv = rbf(mv / (1.f + __expf(-mv)));
    const float r = bf1(x[col]) + rbf(sv * gv);
    ((__bf16*)out)[col] = (__bf16)r;
  }
  if (b == 0 && tid == 0) __hip_atomic_store((gu32*)(ws + WS_EPOCH), tag, __ATOMIC_RELAXED, __HIP_MEMORY_SCOPE_AGENT);
}

}

extern "C" void kernel_launch(void* const* d_in, const int* in_sizes, int n_in, void* d_out, int out_size, void* d_ws, size_t ws_size,
                              hipStream_t stream) {
  const unsigned short* x = (const unsigned short*)d_in[0];
  const unsigned short* kc = (const unsigned short*)d_in[1];
  const unsigned short* vc = (const unsigned short*)d_in[2];
  const unsigned short* wq = (const unsigned short*)d_in[3];
  const unsigned short* wo = (const unsigned short*)d_in[4];
  const unsigned short* w1 = (const unsigned short*)d_in[6];
  const unsigned short* b1 = (const unsigned short*)d_in[7];
  const unsigned short* w2 = (const unsigned short*)d_in[8];
  const unsigned short* b2 = (const unsigned short*)d_in[9];
  fused_decode_kernel<<<256, NTHR, 0, stream>>>(x, kc, vc, wq, wo, w1, b1, w2, b2, (unsigned short*)d_out, (unsigned char*)d_ws);
}
```

```cpp
#include <hip/hip_runtime.h>
#include <hip/hip_bf16.h>
#include <stdint.h>

#define KW 8
#define KD 4
#define KW0 2
#define KW1 2
#define WO_LATE 0
#define KV_NT 1

namespace {

constexpr int DM = 640, NH = 32, SK = 8192, DH = 192, FF = 1536;
constexpr int NSPLIT = 8, NTHR = 512, NWAVE = 8;
constexpr int NG = 16;
constexpr int H1N = 196;
constexpr int H2N = 160;
constexpr size_t WS_EPOCH = 0, WS_H0 = 4096, WS_H1 = WS_H0 + 256 * 2048, WS_H2 = WS_H1 + 256 * 2048, WS_END = WS_H2 + 256 * 2048;

using u32x4 = __attribute__((ext_vector_type(4))) unsigned;
using u32x3 = __attribute__((ext_vector_type(3))) unsigned;
using f32x4 = __attribute__((ext_vector_type(4))) float;
using bf16x2 = __attribute__((ext_vector_type(2))) __bf16;
typedef __attribute__((address_space(1))) unsigned long long gu64;
typedef __attribute__((address_space(1))) unsigned gu32;

__device__ __forceinline__ float rbf(float x) { return (float)(__bf16)x; }
__device__ __forceinline__ float bflo(unsigned u) { return __uint_as_float(u << 16); }
__device__ __forceinline__ float bfhi(unsigned u) { return __uint_as_float(u & 0xffff0000u); }
__device__ __forceinline__ float bf1(unsigned short u) { return __uint_as_float((unsigned)u << 16); }
__device__ __forceinline__ float dot2(unsigned k, unsigned q, float acc) {
  return __builtin_amdgcn_fdot2_f32_bf16(__builtin_bit_cast(bf16x2, k), __builtin_bit_cast(bf16x2, q), acc, false);
}
template <int CTRL> __device__ __forceinline__ float dpp(float x) {
  return __builtin_bit_cast(float, __builtin_amdgcn_mov_dpp(__builtin_bit_cast(int, x), CTRL, 0xf, 0xf, true));
}
constexpr int XOR1 = 0xB1, XOR2 = 0x4E, XOR7 = 0x141, XOR8 = 0x128;
__device__ __forceinline__ float x16_32_sum(float x) {
  auto s = __builtin_amdgcn_permlane16_swap(__float_as_uint(x), __float_as_uint(x), false, false);
  x = __uint_as_float(s[0]) + __uint_as_float(s[1]);
  auto t = __builtin_amdgcn_permlane32_swap(__float_as_uint(x), __float_as_uint(x), false, false);
  return __uint_as_float(t[0]) + __uint_as_float(t[1]);
}
__device__ __forceinline__ float x16_32_max(float x) {
  auto s = __builtin_amdgcn_permlane16_swap(__float_as_uint(x), __float_as_uint(x), false, false);
  x = fmaxf(__uint_as_float(s[0]), __uint_as_float(s[1]));
  auto t = __builtin_amdgcn_permlane32_swap(__float_as_uint(x), __float_as_uint(x), false, false);
  return fmaxf(__uint_as_float(t[0]), __uint_as_float(t[1]));
}
__device__ __forceinline__ float sum8(float x) { x += dpp<XOR1>(x); x += dpp<XOR2>(x); x += dpp<XOR7>(x); return x; }
__device__ __forceinline__ float sumrg(float x) { x += dpp<XOR8>(x); return x16_32_sum(x); }
__device__ __forceinline__ float maxrg(float x) { x = fmaxf(x, dpp<XOR8>(x)); return x16_32_max(x); }
__device__ __forceinline__ float sum64(float x) { return sumrg(sum8(x)); }

__device__ __forceinline__ u32x4 ldkv(const u32x4* p) {
  return __builtin_nontemporal_load(p);
}
__device__ __forceinline__ void st_granule(gu64* g, unsigned tag, float v) {
  __hip_atomic_store(g, ((unsigned long long)tag << 32) | (unsigned long long)__float_as_uint(v), __ATOMIC_RELAXED, __HIP_MEMORY_SCOPE_AGENT);
}
__device__ __forceinline__ unsigned long long ld_granule(gu64* g) {
  return __hip_atomic_load(g, __ATOMIC_RELAXED, __HIP_MEMORY_SCOPE_AGENT);
}
__device__ __forceinline__ void wg_barrier() {
  asm volatile("s_waitcnt lgkmcnt(0)" ::: "memory");
  __builtin_amdgcn_s_barrier();
  asm volatile("" ::: "memory");
}

constexpr unsigned SPIN_CAP = 1u << 22;
#define STAMP(i) do { } while (0)

constexpr int L_XN = 0;
constexpr int L_Y = 640;
constexpr int L_PART = 1280;
constexpr int L_ML = L_PART + 8 * 192;
constexpr int L_HP = L_ML + 32;
constexpr int L_QB = L_HP + 64;
constexpr int L_G1 = L_QB + 96;
constexpr int L_OS = L_G1 + 8 * 240;
constexpr int L_RED = L_OS + 256;
constexpr int L_FIN = L_RED + 48 * 160;
constexpr int L_RH = L_FIN + 160;
constexpr int L_END = L_RH + 80 * 48;

__global__ void __launch_bounds__(NTHR, 2)
fused_decode_kernel(const unsigned short* __restrict__ x, const unsigned short* __restrict__ kc, const unsigned short* __restrict__ vc,
                    const unsigned short* __restrict__ wq, const unsigned short* __restrict__ wo, const unsigned short* __restrict__ w1,
                    const unsigned short* __restrict__ b1, const unsigned short* __restrict__ w2, const unsigned short* __restrict__ b2,
                    unsigned short* __restrict__ out, unsigned char* ws) {
  __shared__ __attribute__((aligned(16))) float lds[L_END];
  const int b = blockIdx.x, xcd = b & 7, hq = (b >> 3) & 3, j = b >> 5, hd = xcd * 4 + hq;
  const int tid = threadIdx.x, lane = tid & 63, wave = __builtin_amdgcn_readfirstlane(tid >> 6);
  const int rg = lane >> 3, cl = lane & 7;

  STAMP(0);
  unsigned xw[5];
#pragma unroll
  for (int i = 0; i < 5; ++i) xw[i] = ((const unsigned*)x)[lane + 64 * i];
  const int tq = tid < 480 ? tid : 479;
  const int qc = tq % 24, qr = tq / 24, hc = tq % 6, hr = tq / 6;
  unsigned short xq[4], xh;
#pragma unroll
  for (int k = 0; k < 4; ++k) xq[k] = x[80 * j + qr + 20 * k];
  xh = x[80 * j + hr];
  u32x4 wqv[4], w1v;
#pragma unroll
  for (int k = 0; k < 4; ++k) wqv[k] = ldkv((const u32x4*)(wq + (size_t)(80 * j + qr + 20 * k) * (NH * DH) + hd * DH + 8 * qc));
  w1v = ldkv((const u32x4*)(w1 + (size_t)(80 * j + hr) * FF + hd * 48 + 8 * hc));
  const unsigned short b1v = b1[hd * 48 + (lane < 48 ? lane : 47)];
  const unsigned E = __hip_atomic_load((gu32*)(ws + WS_EPOCH), __ATOMIC_RELAXED, __HIP_MEMORY_SCOPE_AGENT);
  const u32x4* kp = (const u32x4*)(kc + ((size_t)hd * SK + j * 1024 + wave * 128 + rg) * DH + 8 * cl);
  const u32x4* vp = (const u32x4*)(vc + ((size_t)hd * SK + j * 1024 + wave * 128 + rg) * DH + 8 * cl);
  u32x4 buf[KW][3];
#pragma unroll
  for (int g = 0; g < KW0; ++g)
#pragma unroll
    for (int i = 0; i < 3; ++i) buf[g][i] = ldkv(kp + g * 192 + 8 * i);

  float m1, r1, m2, r2;
  {
    float v[10];
#pragma unroll
    for (int i = 0; i < 5; ++i) { v[2 * i] = bflo(xw[i]); v[2 * i + 1] = bfhi(xw[i]); }
    float s = 0.f;
#pragma unroll
    for (int i = 0; i < 10; ++i) s += v[i];
    float mean = sum64(s) * (1.f / DM), q = 0.f;
#pragma unroll
    for (int i = 0; i < 10; ++i) { const float d = v[i] - mean; q += d * d; }
    float var = rbf(sum64(q) * (1.f / DM));
    r1 = __builtin_amdgcn_rcpf(rbf(__builtin_amdgcn_sqrtf(rbf(var + 1e-5f)))); m1 = rbf(mean);
    s = 0.f;
#pragma unroll
    for (int i = 0; i < 10; ++i) { v[i] = rbf(rbf(v[i] - m1) * r1); s += v[i]; }
    mean = sum64(s) * (1.f / DM); q = 0.f;
#pragma unroll
    for (int i = 0; i < 10; ++i) { const float d = v[i] - mean; q += d * d; }
    var = rbf(sum64(q) * (1.f / DM));
    r2 = __builtin_amdgcn_rcpf(rbf(__builtin_amdgcn_sqrtf(rbf(var + 1e-5f)))); m2 = rbf(mean);
  }
  STAMP(8);
  if (tid < 480) {
    float a[8];
#pragma unroll
    for (int c = 0; c < 8; ++c) a[c] = 0.f;
#pragma unroll
    for (int k = 0; k < 4; ++k) {
      const float xv = rbf(rbf(bf1(xq[k]) - m1) * r1);
#pragma unroll
      for (int c = 0; c < 4; ++c) { a[2 * c] = fmaf(xv, bflo(wqv[k][c]), a[2 * c]); a[2 * c + 1] = fmaf(xv, bfhi(wqv[k][c]), a[2 * c + 1]); }
    }
    float* r = &lds[L_RED + qr * 192 + 8 * qc];
    *(f32x4*)r = f32x4{a[0], a[1], a[2], a[3]}; *(f32x4*)(r + 4) = f32x4{a[4], a[5], a[6], a[7]};
    const float yv = rbf(rbf(rbf(rbf(bf1(xh) - m1) * r1) - m2) * r2);
    float* rh = &lds[L_RH + hr * 48 + 8 * hc];
    *(f32x4*)rh = f32x4{yv * bflo(w1v[0]), yv * bfhi(w1v[0]), yv * bflo(w1v[1]), yv * bfhi(w1v[1])};
    *(f32x4*)(rh + 4) = f32x4{yv * bflo(w1v[2]), yv * bfhi(w1v[2]), yv * bflo(w1v[3]), yv * bfhi(w1v[3])};
  }
  STAMP(9);
  __builtin_amdgcn_sched_barrier(0);
#pragma unroll
  for (int g = KW0; g < KW0 + KW1; ++g)
#pragma unroll
    for (int i = 0; i < 3; ++i) buf[g][i] = ldkv(kp + g * 192 + 8 * i);
  __builtin_amdgcn_sched_barrier(0);
  wg_barrier();
  STAMP(7);
  const unsigned tag = E + 1u;
  gu64* h0 = (gu64*)(ws + WS_H0);
  gu64* h1 = (gu64*)(ws + WS_H1);
  gu64* h2 = (gu64*)(ws + WS_H2);
  if (tid < 192) {
    float s = 0.f;
#pragma unroll
    for (int w = 0; w < 20; ++w) s += lds[L_RED + w * 192 + tid];
    st_granule(h0 + (size_t)b * 256 + tid, tag, s);
  } else if (tid < 240) {
    float s = 0.f;
#pragma unroll
    for (int w = 0; w < 80; ++w) s += lds[L_RH + w * 48 + tid - 192];
    st_granule(h0 + (size_t)b * 256 + tid, tag, s);
  }
  STAMP(10);
  u32x4 wov[4], w2v;
  const int cg = tid % 10, rr = tid / 10;
  int nissued = KW0 + KW1;
  {
    gu64* src = h0 + (size_t)(wave * 32 + hq * 8 + xcd) * 256;
    const int l3 = lane < 48 ? 192 + lane : lane;
    bool done = false;
    auto poll = [&](unsigned long long& g0, unsigned long long& g1, unsigned long long& g2, unsigned long long& g3) {
      g0 = ld_granule(src + lane); g1 = ld_granule(src + 64 + lane); g2 = ld_granule(src + 128 + lane); g3 = ld_granule(src + l3);
    };
    auto check = [&](unsigned long long g0, unsigned long long g1, unsigned long long g2, unsigned long long g3) {
      const bool ok = __all((g0 >> 32) == tag && (g1 >> 32) == tag && (g2 >> 32) == tag && (g3 >> 32) == tag);
      if (ok) {
        lds[L_G1 + wave * 240 + lane] = __uint_as_float((unsigned)g0);
        lds[L_G1 + wave * 240 + 64 + lane] = __uint_as_float((unsigned)g1);
        lds[L_G1 + wave * 240 + 128 + lane] = __uint_as_float((unsigned)g2);
        if (lane < 48) lds[L_G1 + wave * 240 + 192 + lane] = __uint_as_float((unsigned)g3);
      }
      return ok;
    };
    constexpr int NST = (KW - KW0 - KW1) / 2;
#pragma unroll
    for (int st = 0; st < NST; ++st) {
      if (!done) {
        unsigned long long g0, g1, g2, g3;
        poll(g0, g1, g2, g3);
        __builtin_amdgcn_sched_barrier(0);
#pragma unroll
        for (int g = KW0 + KW1 + 2 * st; g < KW0 + KW1 + 2 * st + 2; ++g)
#pragma unroll
          for (int i = 0; i < 3; ++i) buf[g][i] = ldkv(kp + g * 192 + 8 * i);
        __builtin_amdgcn_sched_barrier(0);
        nissued = KW0 + KW1 + 2 * st + 2;
        done = check(g0, g1, g2, g3);
        STAMP(11 + st);
      }
      __builtin_amdgcn_sched_barrier(0);
    }
    if (!done) {
      for (unsigned spins = 0; spins < SPIN_CAP; ++spins) {
        unsigned long long g0, g1, g2, g3;
        poll(g0, g1, g2, g3);
        if (check(g0, g1, g2, g3)) break;
        __builtin_amdgcn_s_sleep(1);
      }
    }
  }
  wg_barrier();
  if (tid < 192) {
    float s = 0.f;
#pragma unroll
    for (int w = 0; w < 8; ++w) s += lds[L_G1 + w * 240 + tid];
    ((__bf16*)&lds[L_QB])[tid] = (__bf16)s;
  } else if (tid >= 256 && tid < 304) {
    float s = 0.f;
#pragma unroll
    for (int w = 0; w < 8; ++w) s += lds[L_G1 + w * 240 + tid - 64];
    const float pre = rbf(rbf(s) + bf1(b1v));
    const float u = 0.7978845608028654f * (pre + 0.044715f * pre * pre * pre);
    lds[L_OS + 192 + tid - 256] = rbf(0.5f * pre * (1.f + tanhf(u)));
  }
  wg_barrier();
  u32x4 qv[3];
#pragma unroll
  for (int i = 0; i < 3; ++i) qv[i] = *(const u32x4*)&lds[L_QB + 4 * (cl + 8 * i)];

  STAMP(1);
  float s[NG];
#pragma unroll
  for (int g = 0; g < NG; ++g) {
    float d = 0.f;
#pragma unroll
    for (int i = 0; i < 3; ++i)
#pragma unroll
      for (int c = 0; c < 4; ++c) d = dot2(buf[g % KW][i][c], qv[i][c], d);
    const int gn = g + KD;
    if (gn >= KW || gn >= nissued) {
#pragma unroll
      for (int i = 0; i < 3; ++i) buf[gn % KW][i] = gn < NG ? ldkv(kp + gn * 192 + 8 * i) : ldkv(vp + (gn - NG) * 192 + 8 * i);
    }
    d = sum8(d);
    s[g] = rbf(rbf(d) * 0.07216878364870322f);
    __builtin_amdgcn_sched_barrier(0);
  }
  float mx = s[0];
#pragma unroll
  for (int g = 1; g < NG; ++g) mx = fmaxf(mx, s[g]);
  mx = maxrg(mx);
  float ls = 0.f;
#pragma unroll
  for (int g = 0; g < NG; ++g) { s[g] = __builtin_amdgcn_exp2f((s[g] - mx) * 1.4426950408889634f); ls += s[g]; }
  ls = sumrg(ls);

  STAMP(2);
  if (tid < 480) {
#pragma unroll
    for (int k = 0; k < 4; ++k) wov[k] = ldkv((const u32x4*)(wo + (size_t)(hd * DH + rr + 48 * k) * DM + 80 * j + 8 * cg));
    w2v = ldkv((const u32x4*)(w2 + (size_t)(hd * 48 + rr) * DM + 80 * j + 8 * cg));
  }
  float acc[24];
#pragma unroll
  for (int k = 0; k < 24; ++k) acc[k] = 0.f;
#pragma unroll
  for (int g = 0; g < NG; ++g) {
    const int G = g + NG;
    const float p = s[g];
#pragma unroll
    for (int i = 0; i < 3; ++i)
#pragma unroll
      for (int c = 0; c < 4; ++c) {
        acc[i * 8 + 2 * c] = fmaf(p, bflo(buf[G % KW][i][c]), acc[i * 8 + 2 * c]);
        acc[i * 8 + 2 * c + 1] = fmaf(p, bfhi(buf[G % KW][i][c]), acc[i * 8 + 2 * c + 1]);
      }
    if (g + KD < NG) {
#pragma unroll
      for (int i = 0; i < 3; ++i) buf[(G + KD) % KW][i] = ldkv(vp + (g + KD) * 192 + 8 * i);
    }
    __builtin_amdgcn_sched_barrier(0);
  }
#pragma unroll
  for (int k = 0; k < 24; ++k) acc[k] = sumrg(acc[k]);
  if (rg == 0) {
#pragma unroll
    for (int i = 0; i < 3; ++i) {
      *(f32x4*)&lds[L_PART + wave * 192 + 8 * (cl + 8 * i)] = f32x4{acc[i * 8], acc[i * 8 + 1], acc[i * 8 + 2], acc[i * 8 + 3]};
      *(f32x4*)&lds[L_PART + wave * 192 + 8 * (cl + 8 * i) + 4] = f32x4{acc[i * 8 + 4], acc[i * 8 + 5], acc[i * 8 + 6], acc[i * 8 + 7]};
    }
    if (cl == 0) { lds[L_ML + wave * 2] = mx; lds[L_ML + wave * 2 + 1] = ls; }
  }
  wg_barrier();
  STAMP(3);
  if (tid < 194) {
    float M = lds[L_ML];
#pragma unroll
    for (int w = 1; w < 8; ++w) M = fmaxf(M, lds[L_ML + 2 * w]);
    float r = 0.f;
    if (tid < 192) {
#pragma unroll
      for (int w = 0; w < 8; ++w) r = fmaf(__builtin_amdgcn_exp2f((lds[L_ML + 2 * w] - M) * 1.4426950408889634f), lds[L_PART + w * 192 + tid], r);
    } else if (tid == 192) {
      r = M;
    } else {
#pragma unroll
      for (int w = 0; w < 8; ++w) r = fmaf(__builtin_amdgcn_exp2f((lds[L_ML + 2 * w] - M) * 1.4426950408889634f), lds[L_ML + 2 * w + 1], r);
    }
    st_granule(h1 + (size_t)b * 256 + tid, tag, r);
  }
  {
    gu64* src = h1 + (size_t)(wave * 32 + hq * 8 + xcd) * 256;
    unsigned long long g0, g1, g2, g3;
    for (unsigned spins = 0; spins < SPIN_CAP; ++spins) {
      g0 = ld_granule(src + lane); g1 = ld_granule(src + 64 + lane); g2 = ld_granule(src + 128 + lane);
      g3 = lane < 2 ? ld_granule(src + 192 + lane) : ((unsigned long long)tag << 32);
      const bool ok = (g0 >> 32) == tag && (g1 >> 32) == tag && (g2 >> 32) == tag && (g3 >> 32) == tag;
      if (__all(ok)) break;
      __builtin_amdgcn_s_sleep(1);
    }
    lds[L_G1 + wave * H1N + lane] = __uint_as_float((unsigned)g0);
    lds[L_G1 + wave * H1N + 64 + lane] = __uint_as_float((unsigned)g1);
    lds[L_G1 + wave * H1N + 128 + lane] = __uint_as_float((unsigned)g2);
    if (lane < 2) lds[L_G1 + wave * H1N + 192 + lane] = __uint_as_float((unsigned)g3);
  }
  wg_barrier();
  STAMP(4);
  if (tid < 192) {
    float M = lds[L_G1 + 192];
#pragma unroll
    for (int w = 1; w < 8; ++w) M = fmaxf(M, lds[L_G1 + w * H1N + 192]);
    float L = 0.f, o = 0.f;
#pragma unroll
    for (int w = 0; w < 8; ++w) {
      const float e = __builtin_amdgcn_exp2f((lds[L_G1 + w * H1N + 192] - M) * 1.4426950408889634f);
      L = fmaf(e, lds[L_G1 + w * H1N + 193], L);
      o = fmaf(e, lds[L_G1 + w * H1N + tid], o);
    }
    lds[L_OS + tid] = rbf(o / L);
  }
  wg_barrier();
  if (tid < 480) {
    float a[8], m[8];
    const float hv = lds[L_OS + 192 + rr];
#pragma unroll
    for (int c = 0; c < 4; ++c) { m[2 * c] = hv * bflo(w2v[c]); m[2 * c + 1] = hv * bfhi(w2v[c]); a[2 * c] = 0.f; a[2 * c + 1] = 0.f; }
#pragma unroll
    for (int k = 0; k < 4; ++k) {
      const float ov = lds[L_OS + rr + 48 * k];
#pragma unroll
      for (int c = 0; c < 4; ++c) { a[2 * c] = fmaf(ov, bflo(wov[k][c]), a[2 * c]); a[2 * c + 1] = fmaf(ov, bfhi(wov[k][c]), a[2 * c + 1]); }
    }
    float* r = &lds[L_RED + rr * 160 + 8 * cg];
    *(f32x4*)r = f32x4{a[0], a[1], a[2], a[3]}; *(f32x4*)(r + 4) = f32x4{a[4], a[5], a[6], a[7]};
    *(f32x4*)(r + 80) = f32x4{m[0], m[1], m[2], m[3]}; *(f32x4*)(r + 84) = f32x4{m[4], m[5], m[6], m[7]};
  }
  wg_barrier();
  if (tid < H2N) {
    float r = 0.f;
#pragma unroll
    for (int k = 0; k < 48; ++k) r += lds[L_RED + k * 160 + tid];
    st_granule(h2 + (size_t)(hd * 8 + j) * 256 + tid, tag, r);
  }
  STAMP(5);
  if (hd != 4 * j) return;
  wg_barrier();
  {
    unsigned long long g[10];
    for (unsigned spins = 0; spins < SPIN_CAP; ++spins) {
      bool ok = true;
#pragma unroll
      for (int k = 0; k < 10; ++k) {
        const int idx = lane + 64 * k, pr = 4 * wave + idx / 160, e = idx % 160;
        g[k] = ld_granule(h2 + (size_t)(pr * 8 + j) * 256 + e);
        ok &= (g[k] >> 32) == tag;
      }
      if (__all(ok)) break;
      __builtin_amdgcn_s_sleep(1);
    }
#pragma unroll
    for (int k = 0; k < 10; ++k) lds[L_RED + wave * 640 + lane + 64 * k] = __uint_as_float((unsigned)g[k]);
  }
  wg_barrier();
  if (tid < H2N) {
    float r = 0.f;
#pragma unroll
    for (int k = 0; k < 32; ++k) r += lds[L_RED + k * 160 + tid];
    lds[L_FIN + tid] = r;
  }
  wg_barrier();
  if (tid < 80) {
    const int col = 80 * j + tid;
    const float sv = rbf(lds[L_FIN + tid]);
    const float mv = rbf(rbf(lds[L_FIN + 80 + tid]) + bf1(b2[col]));
    const float gv = rbf(mv / (1.f + __expf(-mv)));
    const float r = bf1(x[col]) + rbf(sv * gv);
    ((__bf16*)out)[col] = (__bf16)r;
  }
  STAMP(6);
  if (b == 0 && tid == 0) __hip_atomic_store((gu32*)(ws + WS_EPOCH), tag, __ATOMIC_RELAXED, __HIP_MEMORY_SCOPE_AGENT);
}

}

extern "C" void kernel_launch(void* const* d_in, const int* in_sizes, int n_in, void* d_out, int out_size, void* d_ws, size_t ws_size,
                              hipStream_t stream) {
  const unsigned short* x = (const unsigned short*)d_in[0];
  const unsigned short* kc = (const unsigned short*)d_in[1];
  const unsigned short* vc = (const unsigned short*)d_in[2];
  const unsigned short* wq = (const unsigned short*)d_in[3];
  const unsigned short* wo = (const unsigned short*)d_in[4];
  const unsigned short* w1 = (const unsigned short*)d_in[6];
  const unsigned short* b1 = (const unsigned short*)d_in[7];
  const unsigned short* w2 = (const unsigned short*)d_in[8];
  const unsigned short* b2 = (const unsigned short*)d_in[9];
  fused_decode_kernel<<<256, NTHR, 0, stream>>>(x, kc, vc, wq, wo, w1, b1, w2, b2, (unsigned short*)d_out, (unsigned char*)d_ws);
}
```

```cpp
#include <hip/hip_runtime.h>
#include <hip/hip_bf16.h>
#include <stdint.h>

namespace {

constexpr int DM = 640, NH = 32, SK = 8192, DH = 192, FF = 1536;
constexpr int NSPLIT = 8, NTHR = 512, NWAVE = 8;
constexpr int KW = 12;
constexpr int KW0 = 2;
constexpr int KW1 = 2;
constexpr int SG = 2;
constexpr int KD = 4;
constexpr int NG = 16;
constexpr int H1N = 196;
constexpr int H2N = 160;
constexpr size_t WS_EPOCH = 0, WS_H0 = 4096, WS_H1 = WS_H0 + 256 * 2048, WS_H2 = WS_H1 + 256 * 2048, WS_END = WS_H2 + 256 * 2048;

using u32x4 = __attribute__((ext_vector_type(4))) unsigned;
using u32x3 = __attribute__((ext_vector_type(3))) unsigned;
using f32x4 = __attribute__((ext_vector_type(4))) float;
using bf16x2 = __attribute__((ext_vector_type(2))) __bf16;
typedef __attribute__((address_space(1))) unsigned long long gu64;
typedef __attribute__((address_space(1))) unsigned gu32;

__device__ __forceinline__ float rbf(float x) { return (float)(__bf16)x; }
__device__ __forceinline__ float bflo(unsigned u) { return __uint_as_float(u << 16); }
__device__ __forceinline__ float bfhi(unsigned u) { return __uint_as_float(u & 0xffff0000u); }
__device__ __forceinline__ float bf1(unsigned short u) { return __uint_as_float((unsigned)u << 16); }
__device__ __forceinline__ float dot2(unsigned k, unsigned q, float acc) {
  return __builtin_amdgcn_fdot2_f32_bf16(__builtin_bit_cast(bf16x2, k), __builtin_bit_cast(bf16x2, q), acc, false);
}
template <int CTRL> __device__ __forceinline__ float dpp(float x) {
  return __builtin_bit_cast(float, __builtin_amdgcn_mov_dpp(__builtin_bit_cast(int, x), CTRL, 0xf, 0xf, true));
}
constexpr int XOR1 = 0xB1, XOR2 = 0x4E, XOR7 = 0x141, XOR8 = 0x128;
__device__ __forceinline__ float x16_32_sum(float x) {
  auto s = __builtin_amdgcn_permlane16_swap(__float_as_uint(x), __float_as_uint(x), false, false);
  x = __uint_as_float(s[0]) + __uint_as_float(s[1]);
  auto t = __builtin_amdgcn_permlane32_swap(__float_as_uint(x), __float_as_uint(x), false, false);
  return __uint_as_float(t[0]) + __uint_as_float(t[1]);
}
__device__ __forceinline__ float x16_32_max(float x) {
  auto s = __builtin_amdgcn_permlane16_swap(__float_as_uint(x), __float_as_uint(x), false, false);
  x = fmaxf(__uint_as_float(s[0]), __uint_as_float(s[1]));
  auto t = __builtin_amdgcn_permlane32_swap(__float_as_uint(x), __float_as_uint(x), false, false);
  return fmaxf(__uint_as_float(t[0]), __uint_as_float(t[1]));
}
__device__ __forceinline__ float sum8(float x) { x += dpp<XOR1>(x); x += dpp<XOR2>(x); x += dpp<XOR7>(x); return x; }
__device__ __forceinline__ float sumrg(float x) { x += dpp<XOR8>(x); return x16_32_sum(x); }
__device__ __forceinline__ float maxrg(float x) { x = fmaxf(x, dpp<XOR8>(x)); return x16_32_max(x); }
__device__ __forceinline__ float sum64(float x) { return sumrg(sum8(x)); }

__device__ __forceinline__ u32x4 ldkv(const u32x4* p) {
  return __builtin_nontemporal_load(p);
}
__device__ __forceinline__ void st_granule(gu64* g, unsigned tag, float v) {
  __hip_atomic_store(g, ((unsigned long long)tag << 32) | (unsigned long long)__float_as_uint(v), __ATOMIC_RELAXED, __HIP_MEMORY_SCOPE_AGENT);
}
__device__ __forceinline__ unsigned long long ld_granule(gu64* g) {
  return __hip_atomic_load(g, __ATOMIC_RELAXED, __HIP_MEMORY_SCOPE_AGENT);
}
__device__ __forceinline__ void wg_barrier() {
  asm volatile("s_waitcnt lgkmcnt(0)" ::: "memory");
  __builtin_amdgcn_s_barrier();
  asm volatile("" ::: "memory");
}

constexpr unsigned SPIN_CAP = 1u << 22;

constexpr int L_XN = 0;
constexpr int L_Y = 640;
constexpr int L_PART = 1280;
constexpr int L_ML = L_PART + 8 * 192;
constexpr int L_HP = L_ML + 32;
constexpr int L_QB = L_HP + 64;
constexpr int L_G1 = L_QB + 96;
constexpr int L_OS = L_G1 + 8 * 240;
constexpr int L_RED = L_OS + 256;
constexpr int L_FIN = L_RED + 48 * 160;
constexpr int L_RH = L_FIN + 160;
constexpr int L_END = L_RH + 80 * 48;

__global__ void __launch_bounds__(NTHR, 2)
fused_decode_kernel(const unsigned short* __restrict__ x, const unsigned short* __restrict__ kc, const unsigned short* __restrict__ vc,
                    const unsigned short* __restrict__ wq, const unsigned short* __restrict__ wo, const unsigned short* __restrict__ w1,
                    const unsigned short* __restrict__ b1, const unsigned short* __restrict__ w2, const unsigned short* __restrict__ b2,
                    unsigned short* __restrict__ out, unsigned char* ws) {
  __shared__ __attribute__((aligned(16))) float lds[L_END];
  const int b = blockIdx.x, xcd = b & 7, hq = (b >> 3) & 3, j = b >> 5, hd = xcd * 4 + hq;
  const int tid = threadIdx.x, lane = tid & 63, wave = __builtin_amdgcn_readfirstlane(tid >> 6);
  const int rg = lane >> 3, cl = lane & 7;

  unsigned xw[5];
#pragma unroll
  for (int i = 0; i < 5; ++i) xw[i] = ((const unsigned*)x)[lane + 64 * i];
  const int tq = tid < 480 ? tid : 479;
  const int qc = tq % 24, qr = tq / 24, hc = tq % 6, hr = tq / 6;
  unsigned short xq[4], xh;
#pragma unroll
  for (int k = 0; k < 4; ++k) xq[k] = x[80 * j + qr + 20 * k];
  xh = x[80 * j + hr];
  u32x4 wqv[4], w1v;
#pragma unroll
  for (int k = 0; k < 4; ++k) wqv[k] = ldkv((const u32x4*)(wq + (size_t)(80 * j + qr + 20 * k) * (NH * DH) + hd * DH + 8 * qc));
  w1v = ldkv((const u32x4*)(w1 + (size_t)(80 * j + hr) * FF + hd * 48 + 8 * hc));
  const unsigned short b1v = b1[hd * 48 + (lane < 48 ? lane : 47)];
  const unsigned short xfin = x[80 * j + (tid < 80 ? tid : 79)], b2fin = b2[80 * j + (tid < 80 ? tid : 79)];
  const unsigned E = __hip_atomic_load((gu32*)(ws + WS_EPOCH), __ATOMIC_RELAXED, __HIP_MEMORY_SCOPE_AGENT);
  const u32x4* kp = (const u32x4*)(kc + ((size_t)hd * SK + j * 1024 + wave * 128 + rg) * DH + 8 * cl);
  const u32x4* vp = (const u32x4*)(vc + ((size_t)hd * SK + j * 1024 + wave * 128 + rg) * DH + 8 * cl);
  u32x4 buf[KW][3];
#pragma unroll
  for (int g = 0; g < KW0; ++g)
#pragma unroll
    for (int i = 0; i < 3; ++i) buf[g][i] = ldkv(kp + g * 192 + 8 * i);

  float m1, r1, m2, r2;
  {
    float v[10];
#pragma unroll
    for (int i = 0; i < 5; ++i) { v[2 * i] = bflo(xw[i]); v[2 * i + 1] = bfhi(xw[i]); }
    float s = 0.f;
#pragma unroll
    for (int i = 0; i < 10; ++i) s += v[i];
    float mean = sum64(s) * (1.f / DM), q = 0.f;
#pragma unroll
    for (int i = 0; i < 10; ++i) { const float d = v[i] - mean; q += d * d; }
    float var = rbf(sum64(q) * (1.f / DM));
    r1 = __builtin_amdgcn_rcpf(rbf(__builtin_amdgcn_sqrtf(rbf(var + 1e-5f)))); m1 = rbf(mean);
    s = 0.f;
#pragma unroll
    for (int i = 0; i < 10; ++i) { v[i] = rbf(rbf(v[i] - m1) * r1); s += v[i]; }
    mean = sum64(s) * (1.f / DM); q = 0.f;
#pragma unroll
    for (int i = 0; i < 10; ++i) { const float d = v[i] - mean; q += d * d; }
    var = rbf(sum64(q) * (1.f / DM));
    r2 = __builtin_amdgcn_rcpf(rbf(__builtin_amdgcn_sqrtf(rbf(var + 1e-5f)))); m2 = rbf(mean);
  }
  if (tid < 480) {
    float a[8];
#pragma unroll
    for (int c = 0; c < 8; ++c) a[c] = 0.f;
#pragma unroll
    for (int k = 0; k < 4; ++k) {
      const float xv = rbf(rbf(bf1(xq[k]) - m1) * r1);
#pragma unroll
      for (int c = 0; c < 4; ++c) { a[2 * c] = fmaf(xv, bflo(wqv[k][c]), a[2 * c]); a[2 * c + 1] = fmaf(xv, bfhi(wqv[k][c]), a[2 * c + 1]); }
    }
    float* r = &lds[L_RED + qr * 192 + 8 * qc];
    *(f32x4*)r = f32x4{a[0], a[1], a[2], a[3]}; *(f32x4*)(r + 4) = f32x4{a[4], a[5], a[6], a[7]};
    const float yv = rbf(rbf(rbf(rbf(bf1(xh) - m1) * r1) - m2) * r2);
    float* rh = &lds[L_RH + hr * 48 + 8 * hc];
    *(f32x4*)rh = f32x4{yv * bflo(w1v[0]), yv * bfhi(w1v[0]), yv * bflo(w1v[1]), yv * bfhi(w1v[1])};
    *(f32x4*)(rh + 4) = f32x4{yv * bflo(w1v[2]), yv * bfhi(w1v[2]), yv * bflo(w1v[3]), yv * bfhi(w1v[3])};
  }
  __builtin_amdgcn_sched_barrier(0);
#pragma unroll
  for (int g = KW0; g < KW0 + KW1; ++g)
#pragma unroll
    for (int i = 0; i < 3; ++i) buf[g][i] = ldkv(kp + g * 192 + 8 * i);
  __builtin_amdgcn_sched_barrier(0);
  wg_barrier();
  const unsigned tag = E + 1u;
  gu64* h0 = (gu64*)(ws + WS_H0);
  gu64* h1 = (gu64*)(ws + WS_H1);
  gu64* h2 = (gu64*)(ws + WS_H2);
  if (tid < 192) {
    float s = 0.f;
#pragma unroll
    for (int w = 0; w < 20; ++w) s += lds[L_RED + w * 192 + tid];
    st_granule(h0 + (size_t)b * 256 + tid, tag, s);
  } else if (tid < 240) {
    float s = 0.f;
#pragma unroll
    for (int w = 0; w < 80; ++w) s += lds[L_RH + w * 48 + tid - 192];
    st_granule(h0 + (size_t)b * 256 + tid, tag, s);
  }
  u32x4 wov[4], w2v;
  const int cg = tid % 10, rr = tid / 10;
  int nissued = KW0 + KW1;
  {
    gu64* src = h0 + (size_t)(wave * 32 + hq * 8 + xcd) * 256;
    const int l3 = lane < 48 ? 192 + lane : lane;
    bool done = false;
    auto poll = [&](unsigned long long& g0, unsigned long long& g1, unsigned long long& g2, unsigned long long& g3) {
      g0 = ld_granule(src + lane); g1 = ld_granule(src + 64 + lane); g2 = ld_granule(src + 128 + lane); g3 = ld_granule(src + l3);
    };
    auto check = [&](unsigned long long g0, unsigned long long g1, unsigned long long g2, unsigned long long g3) {
      const bool ok = __all((g0 >> 32) == tag && (g1 >> 32) == tag && (g2 >> 32) == tag && (g3 >> 32) == tag);
      if (ok) {
        lds[L_G1 + wave * 240 + lane] = __uint_as_float((unsigned)g0);
        lds[L_G1 + wave * 240 + 64 + lane] = __uint_as_float((unsigned)g1);
        lds[L_G1 + wave * 240 + 128 + lane] = __uint_as_float((unsigned)g2);
        if (lane < 48) lds[L_G1 + wave * 240 + 192 + lane] = __uint_as_float((unsigned)g3);
      }
      return ok;
    };
    constexpr int NST = (KW - KW0 - KW1) / SG;
#pragma unroll
    for (int st = 0; st < NST; ++st) {
      if (!done) {
        unsigned long long g0, g1, g2, g3;
        poll(g0, g1, g2, g3);
        __builtin_amdgcn_sched_barrier(0);
#pragma unroll
        for (int g = KW0 + KW1 + SG * st; g < KW0 + KW1 + SG * st + SG; ++g)
#pragma unroll
          for (int i = 0; i < 3; ++i) buf[g][i] = ldkv(kp + g * 192 + 8 * i);
        __builtin_amdgcn_sched_barrier(0);
        nissued = KW0 + KW1 + SG * st + SG;
        done = check(g0, g1, g2, g3);
      }
      __builtin_amdgcn_sched_barrier(0);
    }
    if (!done) {
      for (unsigned spins = 0; spins < SPIN_CAP; ++spins) {
        unsigned long long g0, g1, g2, g3;
        poll(g0, g1, g2, g3);
        if (check(g0, g1, g2, g3)) break;
        __builtin_amdgcn_s_sleep(1);
      }
    }
  }
  wg_barrier();
  if (tid < 192) {
    float s = 0.f;
#pragma unroll
    for (int w = 0; w < 8; ++w) s += lds[L_G1 + w * 240 + tid];
    ((__bf16*)&lds[L_QB])[tid] = (__bf16)s;
  } else if (tid >= 256 && tid < 304) {
    float s = 0.f;
#pragma unroll
    for (int w = 0; w < 8; ++w) s += lds[L_G1 + w * 240 + tid - 64];
    const float pre = rbf(rbf(s) + bf1(b1v));
    const float u = 0.7978845608028654f * (pre + 0.044715f * pre * pre * pre);
    lds[L_OS + 192 + tid - 256] = rbf(0.5f * pre * (1.f + tanhf(u)));
  }
  wg_barrier();
  u32x4 qv[3];
#pragma unroll
  for (int i = 0; i < 3; ++i) qv[i] = *(const u32x4*)&lds[L_QB + 4 * (cl + 8 * i)];

#pragma unroll
  for (int g = KW0 + KW1; g < KD; ++g) {
    if (g >= nissued) {
#pragma unroll
      for (int i = 0; i < 3; ++i) buf[g][i] = ldkv(kp + g * 192 + 8 * i);
    }
  }
  float s[NG];
#pragma unroll
  for (int g = 0; g < NG; ++g) {
    float d = 0.f;
#pragma unroll
    for (int i = 0; i < 3; ++i)
#pragma unroll
      for (int c = 0; c < 4; ++c) d = dot2(buf[g % KW][i][c], qv[i][c], d);
    const int gn = g + KD;
    if (gn >= KW || gn >= nissued) {
#pragma unroll
      for (int i = 0; i < 3; ++i) buf[gn % KW][i] = gn < NG ? ldkv(kp + gn * 192 + 8 * i) : ldkv(vp + (gn - NG) * 192 + 8 * i);
    }
    d = sum8(d);
    s[g] = rbf(rbf(d) * 0.07216878364870322f);
    __builtin_amdgcn_sched_barrier(0);
  }
  float mx = s[0];
#pragma unroll
  for (int g = 1; g < NG; ++g) mx = fmaxf(mx, s[g]);
  mx = maxrg(mx);
  float ls = 0.f;
#pragma unroll
  for (int g = 0; g < NG; ++g) { s[g] = __builtin_amdgcn_exp2f((s[g] - mx) * 1.4426950408889634f); ls += s[g]; }
  ls = sumrg(ls);

  if (tid < 480) {
#pragma unroll
    for (int k = 0; k < 4; ++k) wov[k] = ldkv((const u32x4*)(wo + (size_t)(hd * DH + rr + 48 * k) * DM + 80 * j + 8 * cg));
    w2v = ldkv((const u32x4*)(w2 + (size_t)(hd * 48 + rr) * DM + 80 * j + 8 * cg));
  }
  float acc[24];
#pragma unroll
  for (int k = 0; k < 24; ++k) acc[k] = 0.f;
#pragma unroll
  for (int g = 0; g < NG; ++g) {
    const int G = g + NG;
    const float p = s[g];
#pragma unroll
    for (int i = 0; i < 3; ++i)
#pragma unroll
      for (int c = 0; c < 4; ++c) {
        acc[i * 8 + 2 * c] = fmaf(p, bflo(buf[G % KW][i][c]), acc[i * 8 + 2 * c]);
        acc[i * 8 + 2 * c + 1] = fmaf(p, bfhi(buf[G % KW][i][c]), acc[i * 8 + 2 * c + 1]);
      }
    if (g + KD < NG) {
#pragma unroll
      for (int i = 0; i < 3; ++i) buf[(G + KD) % KW][i] = ldkv(vp + (g + KD) * 192 + 8 * i);
    }
    __builtin_amdgcn_sched_barrier(0);
  }
#pragma unroll
  for (int k = 0; k < 24; ++k) acc[k] = sumrg(acc[k]);
  if (rg == 0) {
#pragma unroll
    for (int i = 0; i < 3; ++i) {
      *(f32x4*)&lds[L_PART + wave * 192 + 8 * (cl + 8 * i)] = f32x4{acc[i * 8], acc[i * 8 + 1], acc[i * 8 + 2], acc[i * 8 + 3]};
      *(f32x4*)&lds[L_PART + wave * 192 + 8 * (cl + 8 * i) + 4] = f32x4{acc[i * 8 + 4], acc[i * 8 + 5], acc[i * 8 + 6], acc[i * 8 + 7]};
    }
    if (cl == 0) { lds[L_ML + wave * 2] = mx; lds[L_ML + wave * 2 + 1] = ls; }
  }
  wg_barrier();
  if (tid < 194) {
    float M = lds[L_ML];
#pragma unroll
    for (int w = 1; w < 8; ++w) M = fmaxf(M, lds[L_ML + 2 * w]);
    float r = 0.f;
    if (tid < 192) {
#pragma unroll
      for (int w = 0; w < 8; ++w) r = fmaf(__builtin_amdgcn_exp2f((lds[L_ML + 2 * w] - M) * 1.4426950408889634f), lds[L_PART + w * 192 + tid], r);
    } else if (tid == 192) {
      r = M;
    } else {
#pragma unroll
      for (int w = 0; w < 8; ++w) r = fmaf(__builtin_amdgcn_exp2f((lds[L_ML + 2 * w] - M) * 1.4426950408889634f), lds[L_ML + 2 * w + 1], r);
    }
    st_granule(h1 + (size_t)b * 256 + tid, tag, r);
  }
  {
    gu64* src = h1 + (size_t)(wave * 32 + hq * 8 + xcd) * 256;
    unsigned long long g0, g1, g2, g3;
    for (unsigned spins = 0; spins < SPIN_CAP; ++spins) {
      g0 = ld_granule(src + lane); g1 = ld_granule(src + 64 + lane); g2 = ld_granule(src + 128 + lane);
      g3 = lane < 2 ? ld_granule(src + 192 + lane) : ((unsigned long long)tag << 32);
      const bool ok = (g0 >> 32) == tag && (g1 >> 32) == tag && (g2 >> 32) == tag && (g3 >> 32) == tag;
      if (__all(ok)) break;
      __builtin_amdgcn_s_sleep(1);
    }
    lds[L_G1 + wave * H1N + lane] = __uint_as_float((unsigned)g0);
    lds[L_G1 + wave * H1N + 64 + lane] = __uint_as_float((unsigned)g1);
    lds[L_G1 + wave * H1N + 128 + lane] = __uint_as_float((unsigned)g2);
    if (lane < 2) lds[L_G1 + wave * H1N + 192 + lane] = __uint_as_float((unsigned)g3);
  }
  wg_barrier();
  if (tid < 192) {
    float M = lds[L_G1 + 192];
#pragma unroll
    for (int w = 1; w < 8; ++w) M = fmaxf(M, lds[L_G1 + w * H1N + 192]);
    float L = 0.f, o = 0.f;
#pragma unroll
    for (int w = 0; w < 8; ++w) {
      const float e = __builtin_amdgcn_exp2f((lds[L_G1 + w * H1N + 192] - M) * 1.4426950408889634f);
      L = fmaf(e, lds[L_G1 + w * H1N + 193], L);
      o = fmaf(e, lds[L_G1 + w * H1N + tid], o);
    }
    lds[L_OS + tid] = rbf(o / L);
  }
  wg_barrier();
  if (tid < 480) {
    float a[8], m[8];
    const float hv = lds[L_OS + 192 + rr];
#pragma unroll
    for (int c = 0; c < 4; ++c) { m[2 * c] = hv * bflo(w2v[c]); m[2 * c + 1] = hv * bfhi(w2v[c]); a[2 * c] = 0.f; a[2 * c + 1] = 0.f; }
#pragma unroll
    for (int k = 0; k < 4; ++k) {
      const float ov = lds[L_OS + rr + 48 * k];
#pragma unroll
      for (int c = 0; c < 4; ++c) { a[2 * c] = fmaf(ov, bflo(wov[k][c]), a[2 * c]); a[2 * c + 1] = fmaf(ov, bfhi(wov[k][c]), a[2 * c + 1]); }
    }
    float* r = &lds[L_RED + rr * 160 + 8 * cg];
    *(f32x4*)r = f32x4{a[0], a[1], a[2], a[3]}; *(f32x4*)(r + 4) = f32x4{a[4], a[5], a[6], a[7]};
    *(f32x4*)(r + 80) = f32x4{m[0], m[1], m[2], m[3]}; *(f32x4*)(r + 84) = f32x4{m[4], m[5], m[6], m[7]};
  }
  wg_barrier();
  if (tid < H2N) {
    float r = 0.f;
#pragma unroll
    for (int k = 0; k < 48; ++k) r += lds[L_RED + k * 160 + tid];
    st_granule(h2 + (size_t)(hd * 8 + j) * 256 + tid, tag, r);
  }
  if (hd != 4 * j) return;
  wg_barrier();
  {
    unsigned long long g[10];
    for (unsigned spins = 0; spins < SPIN_CAP; ++spins) {
      bool ok = true;
#pragma unroll
      for (int k = 0; k < 10; ++k) {
        const int idx = lane + 64 * k, pr = 4 * wave + idx / 160, e = idx % 160;
        g[k] = ld_granule(h2 + (size_t)(pr * 8 + j) * 256 + e);
        ok &= (g[k] >> 32) == tag;
      }
      if (__all(ok)) break;
      __builtin_amdgcn_s_sleep(1);
    }
#pragma unroll
    for (int k = 0; k < 10; ++k) lds[L_RED + wave * 640 + lane + 64 * k] = __uint_as_float((unsigned)g[k]);
  }
  wg_barrier();
  if (tid < H2N) {
    float r = 0.f;
#pragma unroll
    for (int k = 0; k < 32; ++k) r += lds[L_RED + k * 160 + tid];
    lds[L_FIN + tid] = r;
  }
  wg_barrier();
  if (tid < 80) {
    const int col = 80 * j + tid;
    const float sv = rbf(lds[L_FIN + tid]);
    const float mv = rbf(rbf(lds[L_FIN + 80 + tid]) + bf1(b2fin));
    const float gv = rbf(mv / (1.f + __expf(-mv)));
    const float r = bf1(xfin) + rbf(sv * gv);
    ((__bf16*)out)[col] = (__bf16)r;
  }
  if (b == 0 && tid == 0) __hip_atomic_store((gu32*)(ws + WS_EPOCH), tag, __ATOMIC_RELAXED, __HIP_MEMORY_SCOPE_AGENT);
}

}

extern "C" void kernel_launch(void* const* d_in, const int* in_sizes, int n_in, void* d_out, int out_size, void* d_ws, size_t ws_size,
                              hipStream_t stream) {
  const unsigned short* x = (const unsigned short*)d_in[0];
  const unsigned short* kc = (const unsigned short*)d_in[1];
  const unsigned short* vc = (const unsigned short*)d_in[2];
  const unsigned short* wq = (const unsigned short*)d_in[3];
  const unsigned short* wo = (const unsigned short*)d_in[4];
  const unsigned short* w1 = (const unsigned short*)d_in[6];
  const unsigned short* b1 = (const unsigned short*)d_in[7];
  const unsigned short* w2 = (const unsigned short*)d_in[8];
  const unsigned short* b2 = (const unsigned short*)d_in[9];
  fused_decode_kernel<<<256, NTHR, 0, stream>>>(x, kc, vc, wq, wo, w1, b1, w2, b2, (unsigned short*)d_out, (unsigned char*)d_ws);
}
```
